```python
import torch

def _softmax(x, axis=-1):
    return torch.softmax(x, dim=axis)
def _gelu(x):
    return torch.nn.functional.gelu(x, approximate="tanh")
def _silu(x):
    return torch.nn.functional.silu(x)
def _sigmoid(x):
    return torch.sigmoid(x)
def _layernorm(x, axis=-1, eps=1e-5):
    m = x.mean(dim=axis, keepdim=True)
    v = x.var(dim=axis, keepdim=True, unbiased=False)
    return (x - m) / torch.sqrt(v + eps)
def _scatter_add(tgt, idx, src, axis=0):
    return tgt.clone().index_add_(axis, idx, src)
def _segment_sum(src, seg, n):
    out = torch.zeros((n,) + src.shape[1:], dtype=src.dtype)
    return out.index_add_(0, seg, src)
def _pad(x, pw):
    flat = tuple(v for pair in reversed(pw) for v in pair)
    return torch.nn.functional.pad(x, flat)

kernel_name = 'standalone__softmax_full_bidir__decode'

def setup_inputs():

    _w = (lambda a: (lambda t: t.to(torch.bfloat16) if t.is_floating_point() else t)(torch.as_tensor(a)))
    B, H, Sq, Skv, Dh = 4, 32, 1, 4096, 192
    return {
        "q": _w((lambda *s: torch.randn(*s, dtype=torch.bfloat16) * 0.8)(B, H, Sq, Dh)),
        "k": _w((lambda *s: torch.randn(*s, dtype=torch.bfloat16) * 0.8)(B, H, Skv, Dh)),
        "v": _w((lambda *s: torch.randn(*s, dtype=torch.bfloat16) * 0.05)(B, H, Skv, Dh)),
    }

def reference(q, k, v):
    scale = q.shape[-1] ** -0.5
    scores = torch.einsum('bhqd,bhkd->bhqk', q, k) * scale
    attn = _softmax(scores, axis=-1)
    return torch.einsum('bhqk,bhkd->bhqd', attn, v)
```

```cpp
#include <hip/hip_runtime.h>
#include <hip/hip_bf16.h>
#include <stdint.h>

#define CFG_WAVES 8
#define CFG_NSPLIT 2
#define CFG_RING 2
#define CFG_NT 1

constexpr int B_ = 4, H_ = 32, SKV = 4096, D_ = 192;
constexpr int BH = B_ * H_;
constexpr int WAVES = CFG_WAVES, NTHR = WAVES * 64, NSPLIT = CFG_NSPLIT, RING = CFG_RING;
constexpr int TOK_PER_WG = SKV / NSPLIT;
constexpr int TILE = 8;
constexpr int TILES_PER_WAVE = TOK_PER_WG / (TILE * WAVES);
constexpr int ROWB = D_ * 2;
static_assert(TILES_PER_WAVE % RING == 0, "ring");
constexpr float C2 = 0.07216878364870322f * 1.4426950408889634f;

using bf16x2 = __attribute__((ext_vector_type(2))) __bf16;
using u32x4 = __attribute__((ext_vector_type(4))) unsigned;
using f32x2 = __attribute__((ext_vector_type(2))) float;

__device__ __forceinline__ float dot2(unsigned k, unsigned q, float acc) {
  return __builtin_amdgcn_fdot2_f32_bf16(__builtin_bit_cast(bf16x2, k), __builtin_bit_cast(bf16x2, q), acc, false);
}
template <int CTRL> __device__ __forceinline__ float dpp(float x) {
  return __builtin_bit_cast(float, __builtin_amdgcn_mov_dpp(__builtin_bit_cast(int, x), CTRL, 0xf, 0xf, true));
}
constexpr int XOR1 = 0xB1, XOR2 = 0x4E, XOR7 = 0x141, XOR8 = 0x128;

__device__ __forceinline__ u32x4 ldkv(const unsigned char* p) {
  return __builtin_nontemporal_load(reinterpret_cast<const u32x4*>(p));
}
__device__ __forceinline__ float lo16(unsigned x) { return __builtin_bit_cast(float, x << 16); }
__device__ __forceinline__ float hi16(unsigned x) { return __builtin_bit_cast(float, x & 0xffff0000u); }

__device__ __forceinline__ float swap32_add(float a, float b) {
  auto s = __builtin_amdgcn_permlane32_swap(__float_as_uint(a), __float_as_uint(b), false, false);
  return __uint_as_float(s[0]) + __uint_as_float(s[1]);
}
__device__ __forceinline__ float swap16_add(float a, float b) {
  auto s = __builtin_amdgcn_permlane16_swap(__float_as_uint(a), __float_as_uint(b), false, false);
  return __uint_as_float(s[0]) + __uint_as_float(s[1]);
}
__device__ __forceinline__ float xrow_max(float x) {
  x = fmaxf(x, dpp<XOR8>(x));
  auto s = __builtin_amdgcn_permlane16_swap(__float_as_uint(x), __float_as_uint(x), false, false);
  x = fmaxf(__uint_as_float(s[0]), __uint_as_float(s[1]));
  auto t = __builtin_amdgcn_permlane32_swap(__float_as_uint(x), __float_as_uint(x), false, false);
  return fmaxf(__uint_as_float(t[0]), __uint_as_float(t[1]));
}
__device__ __forceinline__ float xrow_sum(float x) {
  x = x + dpp<XOR8>(x);
  auto s = __builtin_amdgcn_permlane16_swap(__float_as_uint(x), __float_as_uint(x), false, false);
  x = __uint_as_float(s[0]) + __uint_as_float(s[1]);
  auto t = __builtin_amdgcn_permlane32_swap(__float_as_uint(x), __float_as_uint(x), false, false);
  return __uint_as_float(t[0]) + __uint_as_float(t[1]);
}

struct Tile { u32x4 k[3]; u32x4 v[3]; };

__global__ void __launch_bounds__(NTHR)
attn_split(const unsigned char* __restrict__ q, const unsigned char* __restrict__ k, const unsigned char* __restrict__ v,
           float* __restrict__ part) {
  __shared__ __attribute__((aligned(16))) float lds[WAVES * (D_ + 4)];
  const int bh = blockIdx.x / NSPLIT, sp = blockIdx.x % NSPLIT;
  const int tid = threadIdx.x, lane = tid & 63, wave = __builtin_amdgcn_readfirstlane(tid >> 6);
  const int r = lane >> 3, c = lane & 7;
  const size_t base = ((size_t)bh * SKV + (size_t)sp * TOK_PER_WG + wave * TILE) * ROWB;
  const unsigned loff = r * ROWB + c * 16;
  const unsigned char* kp = k + base + loff;
  const unsigned char* vp = v + base + loff;
  constexpr unsigned TSTRIDE = WAVES * TILE * ROWB;

  Tile t[RING];
#pragma unroll
  for (int i = 0; i < RING; ++i) {
#pragma unroll
    for (int j = 0; j < 3; ++j) t[i].k[j] = ldkv(kp + i * TSTRIDE + j * 128);
#pragma unroll
    for (int j = 0; j < 3; ++j) t[i].v[j] = ldkv(vp + i * TSTRIDE + j * 128);
  }
  u32x4 qf[3];
#pragma unroll
  for (int j = 0; j < 3; ++j) qf[j] = *reinterpret_cast<const u32x4*>(q + (size_t)bh * ROWB + j * 128 + c * 16);

  float m = -INFINITY, l = 0.f;
  float acc[24];
#pragma unroll
  for (int i = 0; i < 24; ++i) acc[i] = 0.f;

  for (int it = 0; it < TILES_PER_WAVE; it += RING) {
#pragma unroll
    for (int i = 0; i < RING; ++i) {
      float s0 = 0.f, s1 = 0.f;
#pragma unroll
      for (int j = 0; j < 3; ++j) {
        s0 = dot2(t[i].k[j][0], qf[j][0], s0);
        s1 = dot2(t[i].k[j][1], qf[j][1], s1);
        s0 = dot2(t[i].k[j][2], qf[j][2], s0);
        s1 = dot2(t[i].k[j][3], qf[j][3], s1);
      }
      float s = s0 + s1;
      s += dpp<XOR1>(s);
      s += dpp<XOR2>(s);
      s += dpp<XOR7>(s);
      s *= C2;
      const float mn = fmaxf(m, s);
      const float alpha = __builtin_amdgcn_exp2f(m - mn);
      const float p = __builtin_amdgcn_exp2f(s - mn);
      m = mn;
      l = fmaf(l, alpha, p);
#pragma unroll
      for (int j = 0; j < 3; ++j)
#pragma unroll
        for (int e = 0; e < 4; ++e) {
          const unsigned w = t[i].v[j][e];
          acc[j * 8 + 2 * e] = fmaf(p, lo16(w), acc[j * 8 + 2 * e] * alpha);
          acc[j * 8 + 2 * e + 1] = fmaf(p, hi16(w), acc[j * 8 + 2 * e + 1] * alpha);
        }
      const int nt = it + i + RING;
      if (nt < TILES_PER_WAVE) {
#pragma unroll
        for (int j = 0; j < 3; ++j) t[i].k[j] = ldkv(kp + (size_t)nt * TSTRIDE + j * 128);
#pragma unroll
        for (int j = 0; j < 3; ++j) t[i].v[j] = ldkv(vp + (size_t)nt * TSTRIDE + j * 128);
      }
    }
  }
  const float mw = xrow_max(m);
  const float a = __builtin_amdgcn_exp2f(m - mw);
  l = xrow_sum(l * a);
#pragma unroll
  for (int i = 0; i < 24; ++i) acc[i] *= a;
  float y[12], z[6];
#pragma unroll
  for (int i = 0; i < 12; ++i) y[i] = swap32_add(acc[i], acc[i + 12]);
#pragma unroll
  for (int i = 0; i < 6; ++i) z[i] = swap16_add(y[i], y[i + 6]);
#pragma unroll
  for (int i = 0; i < 6; ++i) z[i] += dpp<XOR8>(z[i]);
  float* rec = lds + wave * (D_ + 4);
  if ((lane & 8) == 0) {
    const int a0 = 6 * ((lane >> 4) & 1) + 12 * (lane >> 5);
#pragma unroll
    for (int i = 0; i < 6; ++i) { const int ai = a0 + i; rec[4 + 64 * (ai >> 3) + 8 * c + (ai & 7)] = z[i]; }
  }
  if (lane == 0) { rec[0] = mw; rec[1] = l; }
  __syncthreads();
  if (tid < D_) {
    float M = -INFINITY;
#pragma unroll
    for (int w = 0; w < WAVES; ++w) M = fmaxf(M, lds[w * (D_ + 4)]);
    float L = 0.f, o = 0.f;
#pragma unroll
    for (int w = 0; w < WAVES; ++w) {
      const float wt = __builtin_amdgcn_exp2f(lds[w * (D_ + 4)] - M);
      L = fmaf(wt, lds[w * (D_ + 4) + 1], L);
      o = fmaf(wt, lds[w * (D_ + 4) + 4 + tid], o);
    }
    float* pr = part + (size_t)blockIdx.x * 256;
    pr[tid] = o;
    if (tid == 0) { pr[192] = M; pr[193] = L; }
  }
}

__global__ void __launch_bounds__(D_)
attn_combine(const float* __restrict__ part, __hip_bfloat16* __restrict__ out) {
  const int bh = blockIdx.x, d = threadIdx.x;
  const float* pr = part + (size_t)bh * NSPLIT * 256;
  float M = -INFINITY;
#pragma unroll
  for (int s = 0; s < NSPLIT; ++s) M = fmaxf(M, pr[s * 256 + 192]);
  float L = 0.f, o = 0.f;
#pragma unroll
  for (int s = 0; s < NSPLIT; ++s) {
    const float wt = __builtin_amdgcn_exp2f(pr[s * 256 + 192] - M);
    L = fmaf(wt, pr[s * 256 + 193], L);
    o = fmaf(wt, pr[s * 256 + d], o);
  }
  out[bh * D_ + d] = __float2bfloat16(o / L);
}

extern "C" void kernel_launch(void* const* d_in, const int* in_sizes, int n_in, void* d_out, int out_size, void* d_ws,
                              size_t ws_size, hipStream_t stream) {
  const unsigned char* q = (const unsigned char*)d_in[0];
  const unsigned char* k = (const unsigned char*)d_in[1];
  const unsigned char* v = (const unsigned char*)d_in[2];
  float* part = (float*)d_ws;
  attn_split<<<BH * NSPLIT, NTHR, 0, stream>>>(q, k, v, part);
  attn_combine<<<BH, D_, 0, stream>>>(part, (__hip_bfloat16*)d_out);
}
```

```cpp
#include <hip/hip_runtime.h>
#include <hip/hip_bf16.h>
#include <stdint.h>

constexpr int B_ = 4, H_ = 32, SKV = 4096, D_ = 192;
constexpr int BH = B_ * H_, NWG = 2 * BH;
constexpr int WAVES = 4, NTHR = (WAVES + 1) * 64;
constexpr int KEYS_PER_WG = SKV / 2;
constexpr int TILE = 8;
constexpr int T = KEYS_PER_WG / (TILE * WAVES);
constexpr int ROWB = D_ * 2;
constexpr unsigned ROUNDB = WAVES * TILE * ROWB;
constexpr unsigned KVBYTES = (unsigned)BH * SKV * ROWB;
constexpr int NT_LOAD = 2;
constexpr float C2 = 0.07216878364870322f * 1.4426950408889634f;
static_assert((T & (T - 1)) == 0, "cyclic tile order uses a mask");

using bf16x2 = __attribute__((ext_vector_type(2))) __bf16;
using u32x4 = __attribute__((ext_vector_type(4))) unsigned;
using f32x2 = __attribute__((ext_vector_type(2))) float;
using f32x4 = __attribute__((ext_vector_type(4))) float;

__device__ __forceinline__ float dot2(unsigned k, unsigned q, float acc) {
  return __builtin_amdgcn_fdot2_f32_bf16(__builtin_bit_cast(bf16x2, k), __builtin_bit_cast(bf16x2, q), acc, false);
}
template <int CTRL> __device__ __forceinline__ float dpp(float x) {
  return __builtin_bit_cast(float, __builtin_amdgcn_mov_dpp(__builtin_bit_cast(int, x), CTRL, 0xf, 0xf, true));
}
constexpr int XOR1 = 0xB1, XOR2 = 0x4E, XOR7 = 0x141, XOR8 = 0x128, ODD = 0xF5;
__device__ __forceinline__ float lo16(unsigned x) { return __builtin_bit_cast(float, x << 16); }
__device__ __forceinline__ float hi16(unsigned x) { return __builtin_bit_cast(float, x & 0xffff0000u); }
__device__ __forceinline__ float swap32_add(float a, float b) {
  auto s = __builtin_amdgcn_permlane32_swap(__float_as_uint(a), __float_as_uint(b), false, false);
  return __uint_as_float(s[0]) + __uint_as_float(s[1]);
}
__device__ __forceinline__ float swap16_add(float a, float b) {
  auto s = __builtin_amdgcn_permlane16_swap(__float_as_uint(a), __float_as_uint(b), false, false);
  return __uint_as_float(s[0]) + __uint_as_float(s[1]);
}
__device__ __forceinline__ float xrow_max(float x) {
  x = fmaxf(x, dpp<XOR8>(x));
  auto s = __builtin_amdgcn_permlane16_swap(__float_as_uint(x), __float_as_uint(x), false, false);
  x = fmaxf(__uint_as_float(s[0]), __uint_as_float(s[1]));
  auto t = __builtin_amdgcn_permlane32_swap(__float_as_uint(x), __float_as_uint(x), false, false);
  return fmaxf(__uint_as_float(t[0]), __uint_as_float(t[1]));
}
__device__ __forceinline__ float xrow_sum(float x) {
  x = x + dpp<XOR8>(x);
  auto s = __builtin_amdgcn_permlane16_swap(__float_as_uint(x), __float_as_uint(x), false, false);
  x = __uint_as_float(s[0]) + __uint_as_float(s[1]);
  auto t = __builtin_amdgcn_permlane32_swap(__float_as_uint(x), __float_as_uint(x), false, false);
  return __uint_as_float(t[0]) + __uint_as_float(t[1]);
}

typedef __attribute__((address_space(1))) unsigned long long gu64;
typedef __attribute__((address_space(3))) volatile unsigned lds_u32;
constexpr unsigned LINE = 128, BOX_OFF = NWG * LINE, BOXB = 1024;
constexpr unsigned EMPTY = 0xFFFFFFFFu;
constexpr unsigned SPIN_CAP = 1u << 22;
constexpr int REC = BOXB / 4;

__device__ __forceinline__ unsigned long long ld_agent(gu64* p) { return __hip_atomic_load(p, __ATOMIC_RELAXED, __HIP_MEMORY_SCOPE_AGENT); }
__device__ __forceinline__ void st_agent(gu64* p, unsigned long long v) { __hip_atomic_store(p, v, __ATOMIC_RELAXED, __HIP_MEMORY_SCOPE_AGENT); }
__device__ __forceinline__ unsigned uni(unsigned x) { return __builtin_amdgcn_readfirstlane(x); }
__device__ __forceinline__ bool box_full(const u32x4& b) { return __all(b[0] != EMPTY && b[1] != EMPTY && b[2] != EMPTY && b[3] != EMPTY); }
#define BOX_LOAD(dst, ptr) asm volatile("global_load_dwordx4 %0, %1, off sc1" : "=&v"(dst) : "v"(ptr) : "memory")
#define BOX_STORE(ptr, src) asm volatile("global_store_dwordx4 %0, %1, off sc1\n\ts_nop 1" :: "v"(ptr), "v"(src) : "memory")
#define BOX_WAIT(n, dst) asm volatile("s_waitcnt vmcnt(" #n ")" : "+v"(dst) :: "memory")
#define DRAIN() asm volatile("s_waitcnt vmcnt(0)" ::: "memory")
#define SB() __builtin_amdgcn_sched_barrier(0)

__global__ void __launch_bounds__(NTHR, 2)
attn_decode(const unsigned char* __restrict__ q, const unsigned char* __restrict__ k, const unsigned char* __restrict__ v,
            unsigned char* __restrict__ ws, unsigned char* __restrict__ out) {
  __shared__ __attribute__((aligned(16))) float lds[WAVES * REC + 4];
  lds_u32* done = (lds_u32*)&lds[WAVES * REC];
  const int wg = blockIdx.x, bh = wg >> 1;
  const int tid = threadIdx.x, lane = tid & 63, wave = __builtin_amdgcn_readfirstlane(tid >> 6);

  if (wave == WAVES) {
    if (lane == 0) *done = 0u;
    asm volatile("s_waitcnt lgkmcnt(0)\n\ts_barrier" ::: "memory");
    gu64* hme = (gu64*)(ws + wg * LINE);
    gu64* hpa = (gu64*)(ws + (wg ^ 1) * LINE);
    const unsigned char* mbox = ws + BOX_OFF + wg * BOXB + lane * 16;
    const unsigned char* pbox = ws + BOX_OFF + (wg ^ 1) * BOXB + lane * 16;
    const int slot = lane >> 1, a0 = 6 * (slot >> 3) + 4 * (lane & 1), a1 = a0 + 2;
    unsigned char* op0 = out + (bh * D_ + 64 * (a0 >> 3) + 8 * (slot & 7) + (a0 & 7)) * 2;
    unsigned char* op1 = out + (bh * D_ + 64 * (a1 >> 3) + 8 * (slot & 7) + (a1 & 7)) * 2;
    unsigned touch;
    asm volatile("global_load_dword %0, %1, off nt" : "=v"(touch) : "v"(op0) : "memory");

    const unsigned long long own = ld_agent(hme), par0 = ld_agent(hpa);
    const unsigned n_old = uni((unsigned)own), pe0 = uni((unsigned)(par0 >> 32));
    unsigned echoed = uni((unsigned)par0);
    unsigned n = n_old + 1u;
    if (n == pe0) n += 1u;
    BOX_STORE(mbox, (u32x4{EMPTY, EMPTY, EMPTY, EMPTY}));
    asm volatile("s_waitcnt vmcnt(0)" :: "v"(touch) : "memory");
    if (lane == 0) st_agent(hme, ((unsigned long long)echoed << 32) | n);
    bool ok = false;
    for (unsigned spins = 0; spins < SPIN_CAP; ++spins) {
      const unsigned long long par = ld_agent(hpa);
      const unsigned pn = uni((unsigned)par), pe = uni((unsigned)(par >> 32));
      if (pn != echoed) {
        DRAIN();
        if (lane == 0) st_agent(hme, ((unsigned long long)pn << 32) | n);
        echoed = pn;
      }
      if (pe == n) { ok = true; break; }
      __builtin_amdgcn_s_sleep(8);
    }

    while (uni(*done) == 0u) __builtin_amdgcn_s_sleep(4);
    u32x4 pb;
    bool full = false, pending;
    for (;;) {
      BOX_LOAD(pb, pbox);
      pending = true;
      bool all = false;
      for (int i = 0; i < 6 && !all; ++i) all = uni(*done) == (unsigned)WAVES;
      if (all) break;
      BOX_WAIT(0, pb);
      pending = false;
      full = box_full(pb);
      if (full) { while (uni(*done) != (unsigned)WAVES) {} break; }
    }
    asm volatile("" ::: "memory");

    f32x4 x[WAVES];
    float mw[WAVES], M = -INFINITY;
#pragma unroll
    for (int w = 0; w < WAVES; ++w) x[w] = *reinterpret_cast<const f32x4*>(&lds[w * REC + lane * 4]);
#pragma unroll
    for (int w = 0; w < WAVES; ++w) { mw[w] = dpp<ODD>(x[w][2]); M = fmaxf(M, mw[w]); }
    f32x4 o = {0.f, 0.f, 0.f, 0.f};
#pragma unroll
    for (int w = 0; w < WAVES; ++w) o += __builtin_amdgcn_exp2f(mw[w] - M) * x[w];
    if (lane & 1) o[2] = M;
    asm volatile("" : "+v"(o));
    if (pending) { BOX_WAIT(0, pb); full = box_full(pb); }

    if (ok) {
      if (!full) {
        u32x4 mine = __builtin_bit_cast(u32x4, o);
#pragma unroll
        for (int i = 0; i < 4; ++i) mine[i] = mine[i] == EMPTY ? 0x7FFFFFFFu : mine[i];
        BOX_LOAD(pb, pbox);
        BOX_STORE(mbox, mine);
        BOX_WAIT(1, pb);
        if (!box_full(pb)) {
          DRAIN();
          BOX_LOAD(pb, pbox);
          BOX_WAIT(0, pb);
          if (!box_full(pb)) return;
        }
      }
    } else {
      pb = u32x4{0x7FC00000u, 0x7FC00000u, 0x7FC00000u, 0x7FC00000u};
    }

    const f32x4 c = __builtin_bit_cast(f32x4, pb);
    const float M1 = dpp<ODD>(c[2]), L1 = dpp<ODD>(c[3]), L0 = dpp<ODD>(o[3]);
    const float Mf = fmaxf(M, M1), w0 = __builtin_amdgcn_exp2f(M - Mf), w1 = __builtin_amdgcn_exp2f(M1 - Mf);
    float t0 = w0 * L0, t1 = w1 * L1;
    asm volatile("" : "+v"(t0), "+v"(t1));
    const float inv = 1.0f / (t0 + t1);
    unsigned h[4];
#pragma unroll
    for (int i = 0; i < 4; ++i) {
      float u0 = w0 * o[i], u1 = w1 * c[i];
      asm volatile("" : "+v"(u0), "+v"(u1));
      h[i] = __builtin_bit_cast(unsigned short, __float2bfloat16((u0 + u1) * inv));
    }
    *reinterpret_cast<unsigned*>(op0) = h[0] | (h[1] << 16);
    if ((lane & 1) == 0) *reinterpret_cast<unsigned*>(op1) = h[2] | (h[3] << 16);
    return;
  }

  const int r = lane >> 3, c = lane & 7;
  const auto rk = __builtin_amdgcn_make_buffer_rsrc((void*)k, 0, KVBYTES, 0x00020000);
  const auto rv = __builtin_amdgcn_make_buffer_rsrc((void*)v, 0, KVBYTES, 0x00020000);
  const unsigned wbase = (unsigned)(wg * KEYS_PER_WG + wave * TILE) * ROWB;
  const unsigned loff = r * ROWB + c * 16;
  const unsigned j0 = (((((unsigned)bh * 2654435761u) >> 16) * (unsigned)T) >> 16) + (wg & 1) * (T / 2);
#define TILE_OFF(t) (wbase + ((j0 + (t)) & (T - 1)) * ROUNDB)

  u32x4 qf[3], kr[3], vr[3];
#pragma unroll
  for (int j = 0; j < 3; ++j) qf[j] = *reinterpret_cast<const u32x4*>(q + (size_t)bh * ROWB + j * 128 + c * 16);
  SB();
#define LOAD3(dst, rsrc, so) do { const unsigned so_ = (so); _Pragma("unroll") for (int j_ = 0; j_ < 3; ++j_) { \
    dst[j_] = __builtin_amdgcn_raw_buffer_load_b128(rsrc, loff + j_ * 128, so_, NT_LOAD); SB(); } } while (0)
  LOAD3(kr, rk, TILE_OFF(0));
  LOAD3(vr, rv, TILE_OFF(0));
  asm volatile("s_barrier" ::: "memory");

  float m = -INFINITY, l = 0.f, p, alpha, sc;
  f32x2 acc[12];
#pragma unroll
  for (int i = 0; i < 12; ++i) acc[i] = f32x2{0.f, 0.f};

#define QK() do { float s0 = 0.f, s1 = 0.f; \
    _Pragma("unroll") for (int j_ = 0; j_ < 3; ++j_) { \
      s0 = dot2(kr[j_][0], qf[j_][0], s0); s1 = dot2(kr[j_][1], qf[j_][1], s1); \
      s0 = dot2(kr[j_][2], qf[j_][2], s0); s1 = dot2(kr[j_][3], qf[j_][3], s1); } \
    sc = s0 + s1; asm volatile("" : "+v"(sc)); } while (0)
#define SOFTMAX() do { float s_ = sc; s_ += dpp<XOR1>(s_); s_ += dpp<XOR2>(s_); s_ += dpp<XOR7>(s_); s_ *= C2; \
    const float mn_ = fmaxf(m, xrow_max(s_)); alpha = __builtin_amdgcn_exp2f(m - mn_); p = __builtin_amdgcn_exp2f(s_ - mn_); \
    m = mn_; l = fmaf(l, alpha, p); } while (0)
#define PV() do { const f32x2 a2_ = {alpha, alpha}, p2_ = {p, p}; \
    _Pragma("unroll") for (int j_ = 0; j_ < 3; ++j_) _Pragma("unroll") for (int e_ = 0; e_ < 4; ++e_) { \
      const unsigned w_ = vr[j_][e_]; const f32x2 v2_ = {lo16(w_), hi16(w_)}; \
      acc[j_ * 4 + e_] = __builtin_elementwise_fma(p2_, v2_, acc[j_ * 4 + e_] * a2_); } } while (0)

  for (int t = 1; t < T; ++t) {
    const unsigned so = TILE_OFF(t);
    QK(); SB();
    LOAD3(kr, rk, so);
    SOFTMAX(); PV(); SB();
    LOAD3(vr, rv, so);
  }
  QK(); SOFTMAX(); PV();

  l = xrow_sum(l);
  float y[12], z[6];
#pragma unroll
  for (int i = 0; i < 12; ++i)
    y[i] = swap32_add(acc[i / 2][i % 2], acc[(i + 12) / 2][i % 2]);
#pragma unroll
  for (int i = 0; i < 6; ++i) z[i] = swap16_add(y[i], y[i + 6]);
#pragma unroll
  for (int i = 0; i < 6; ++i) z[i] += dpp<XOR8>(z[i]);
  const bool hi = lane & 8;
  const f32x4 rec = {hi ? z[4] : z[0], hi ? z[5] : z[1], hi ? m : z[2], hi ? l : z[3]};
  *reinterpret_cast<f32x4*>(&lds[wave * REC + (8 * (lane >> 4) + c) * 8 + (lane & 8) / 2]) = rec;
  asm volatile("" ::: "memory");
  if (lane == 0) __hip_atomic_fetch_add((__attribute__((address_space(3))) unsigned*)done, 1u, __ATOMIC_RELAXED, __HIP_MEMORY_SCOPE_WORKGROUP);
}

extern "C" void kernel_launch(void* const* d_in, const int* in_sizes, int n_in, void* d_out, int out_size, void* d_ws,
                              size_t ws_size, hipStream_t stream) {
  attn_decode<<<NWG, NTHR, 0, stream>>>((const unsigned char*)d_in[0], (const unsigned char*)d_in[1], (const unsigned char*)d_in[2],
                                        (unsigned char*)d_ws, (unsigned char*)d_out);
}
```

```cpp
#include <hip/hip_runtime.h>
#include <hip/hip_bf16.h>
#include <stdint.h>

constexpr int B_ = 4, H_ = 32, SKV = 4096, D_ = 192;
constexpr int BH = B_ * H_, NWG = 2 * BH;
constexpr int WAVES = 4, NTHR = (WAVES + 1) * 64;
constexpr int KEYS_PER_WG = SKV / 2;
constexpr int TILE = 8;
constexpr int T = KEYS_PER_WG / (TILE * WAVES);
constexpr int ROWB = D_ * 2;
constexpr unsigned ROUNDB = WAVES * TILE * ROWB;
constexpr unsigned KVBYTES = (unsigned)BH * SKV * ROWB;
constexpr int NT_LOAD = 2;
constexpr int K_PACE = 4;
constexpr int V_DEEP = 24;
static_assert(V_DEEP % 6 == 0, "two- and three-tile loops");
constexpr float C2 = 0.07216878364870322f * 1.4426950408889634f;
static_assert((T & (T - 1)) == 0, "cyclic tile order uses a mask");

using bf16x2 = __attribute__((ext_vector_type(2))) __bf16;
using u32x4 = __attribute__((ext_vector_type(4))) unsigned;
using f32x2 = __attribute__((ext_vector_type(2))) float;
using f32x4 = __attribute__((ext_vector_type(4))) float;

__device__ __forceinline__ float dot2(unsigned k, unsigned q, float acc) {
  return __builtin_amdgcn_fdot2_f32_bf16(__builtin_bit_cast(bf16x2, k), __builtin_bit_cast(bf16x2, q), acc, false);
}
template <int CTRL> __device__ __forceinline__ float dpp(float x) {
  return __builtin_bit_cast(float, __builtin_amdgcn_mov_dpp(__builtin_bit_cast(int, x), CTRL, 0xf, 0xf, true));
}
constexpr int XOR1 = 0xB1, XOR2 = 0x4E, XOR7 = 0x141, XOR8 = 0x128, ODD = 0xF5;
__device__ __forceinline__ float lo16(unsigned x) { return __builtin_bit_cast(float, x << 16); }
__device__ __forceinline__ float hi16(unsigned x) { return __builtin_bit_cast(float, x & 0xffff0000u); }
__device__ __forceinline__ float swap32_add(float a, float b) {
  auto s = __builtin_amdgcn_permlane32_swap(__float_as_uint(a), __float_as_uint(b), false, false);
  return __uint_as_float(s[0]) + __uint_as_float(s[1]);
}
__device__ __forceinline__ float swap16_add(float a, float b) {
  auto s = __builtin_amdgcn_permlane16_swap(__float_as_uint(a), __float_as_uint(b), false, false);
  return __uint_as_float(s[0]) + __uint_as_float(s[1]);
}
__device__ __forceinline__ float xrow_max(float x) {
  x = fmaxf(x, dpp<XOR8>(x));
  auto s = __builtin_amdgcn_permlane16_swap(__float_as_uint(x), __float_as_uint(x), false, false);
  x = fmaxf(__uint_as_float(s[0]), __uint_as_float(s[1]));
  auto t = __builtin_amdgcn_permlane32_swap(__float_as_uint(x), __float_as_uint(x), false, false);
  return fmaxf(__uint_as_float(t[0]), __uint_as_float(t[1]));
}
__device__ __forceinline__ float xrow_sum(float x) {
  x = x + dpp<XOR8>(x);
  auto s = __builtin_amdgcn_permlane16_swap(__float_as_uint(x), __float_as_uint(x), false, false);
  x = __uint_as_float(s[0]) + __uint_as_float(s[1]);
  auto t = __builtin_amdgcn_permlane32_swap(__float_as_uint(x), __float_as_uint(x), false, false);
  return __uint_as_float(t[0]) + __uint_as_float(t[1]);
}

typedef __attribute__((address_space(1))) unsigned long long gu64;
typedef __attribute__((address_space(3))) volatile unsigned lds_u32;
constexpr unsigned LINE = 128, BOX_OFF = NWG * LINE, BOXB = 1024;
constexpr unsigned EMPTY = 0xFFFFFFFFu;
constexpr unsigned SPIN_CAP = 1u << 22;
constexpr int REC = BOXB / 4;

__device__ __forceinline__ unsigned long long ld_agent(gu64* p) { return __hip_atomic_load(p, __ATOMIC_RELAXED, __HIP_MEMORY_SCOPE_AGENT); }
__device__ __forceinline__ void st_agent(gu64* p, unsigned long long v) { __hip_atomic_store(p, v, __ATOMIC_RELAXED, __HIP_MEMORY_SCOPE_AGENT); }
__device__ __forceinline__ unsigned uni(unsigned x) { return __builtin_amdgcn_readfirstlane(x); }
__device__ __forceinline__ bool box_full(const u32x4& b) { return __all(b[0] != EMPTY && b[1] != EMPTY && b[2] != EMPTY && b[3] != EMPTY); }
#define BOX_LOAD(dst, ptr) asm volatile("global_load_dwordx4 %0, %1, off sc1" : "=&v"(dst) : "v"(ptr) : "memory")
#define BOX_STORE(ptr, src) asm volatile("global_store_dwordx4 %0, %1, off sc1\n\ts_nop 1" :: "v"(ptr), "v"(src) : "memory")
#define BOX_WAIT(n, dst) asm volatile("s_waitcnt vmcnt(" #n ")" : "+v"(dst) :: "memory")
#define DRAIN() asm volatile("s_waitcnt vmcnt(0)" ::: "memory")
#define SB() __builtin_amdgcn_sched_barrier(0)

__global__ void __launch_bounds__(NTHR, 2)
attn_decode(const unsigned char* __restrict__ q, const unsigned char* __restrict__ k, const unsigned char* __restrict__ v,
            unsigned char* __restrict__ ws, unsigned char* __restrict__ out) {
  __shared__ __attribute__((aligned(16))) float lds[WAVES * REC + 4 + WAVES * T * TILE];
  lds_u32* done = (lds_u32*)&lds[WAVES * REC];
  const int wg = blockIdx.x, bh = wg >> 1;
  const int tid = threadIdx.x, lane = tid & 63, wave = __builtin_amdgcn_readfirstlane(tid >> 6);

  if (wave == WAVES) {
    if (lane == 0) *done = 0u;
    asm volatile("s_waitcnt lgkmcnt(0)\n\ts_barrier" ::: "memory");
    gu64* hme = (gu64*)(ws + wg * LINE);
    gu64* hpa = (gu64*)(ws + (wg ^ 1) * LINE);
    const unsigned char* mbox = ws + BOX_OFF + wg * BOXB + lane * 16;
    const unsigned char* pbox = ws + BOX_OFF + (wg ^ 1) * BOXB + lane * 16;
    const int slot = lane >> 1, a0 = 6 * (slot >> 3) + 4 * (lane & 1), a1 = a0 + 2;
    unsigned char* op0 = out + (bh * D_ + 64 * (a0 >> 3) + 8 * (slot & 7) + (a0 & 7)) * 2;
    unsigned char* op1 = out + (bh * D_ + 64 * (a1 >> 3) + 8 * (slot & 7) + (a1 & 7)) * 2;

    const unsigned long long own = ld_agent(hme), par0 = ld_agent(hpa);
    const unsigned n_old = uni((unsigned)own), pe0 = uni((unsigned)(par0 >> 32));
    unsigned echoed = uni((unsigned)par0);
    unsigned n = n_old + 1u;
    if (n == pe0) n += 1u;
    BOX_STORE(mbox, (u32x4{EMPTY, EMPTY, EMPTY, EMPTY}));
    DRAIN();
    if (lane == 0) st_agent(hme, ((unsigned long long)echoed << 32) | n);
    bool ok = false;
    for (unsigned spins = 0; spins < SPIN_CAP; ++spins) {
      const unsigned long long par = ld_agent(hpa);
      const unsigned pn = uni((unsigned)par), pe = uni((unsigned)(par >> 32));
      if (pn != echoed) {
        DRAIN();
        if (lane == 0) st_agent(hme, ((unsigned long long)pn << 32) | n);
        echoed = pn;
      }
      if (pe == n) { ok = true; break; }
      __builtin_amdgcn_s_sleep(8);
    }

    while (uni(*done) == 0u) __builtin_amdgcn_s_sleep(4);
    u32x4 pb;
    bool full = false, pending;
    for (;;) {
      BOX_LOAD(pb, pbox);
      pending = true;
      bool all = false;
      for (int i = 0; i < 6 && !all; ++i) all = uni(*done) == (unsigned)WAVES;
      if (all) break;
      BOX_WAIT(0, pb);
      pending = false;
      full = box_full(pb);
      if (full) { while (uni(*done) != (unsigned)WAVES) {} break; }
    }
    asm volatile("" ::: "memory");

    f32x4 x[WAVES];
    float mw[WAVES], M = -INFINITY;
#pragma unroll
    for (int w = 0; w < WAVES; ++w) x[w] = *reinterpret_cast<const f32x4*>(&lds[w * REC + lane * 4]);
#pragma unroll
    for (int w = 0; w < WAVES; ++w) { mw[w] = dpp<ODD>(x[w][2]); M = fmaxf(M, mw[w]); }
    f32x4 o = {0.f, 0.f, 0.f, 0.f};
#pragma unroll
    for (int w = 0; w < WAVES; ++w) o += __builtin_amdgcn_exp2f(mw[w] - M) * x[w];
    if (lane & 1) o[2] = M;
    asm volatile("" : "+v"(o));
    if (pending) { BOX_WAIT(0, pb); full = box_full(pb); }

    if (ok) {
      if (!full) {
        u32x4 mine = __builtin_bit_cast(u32x4, o);
#pragma unroll
        for (int i = 0; i < 4; ++i) mine[i] = mine[i] == EMPTY ? 0x7FFFFFFFu : mine[i];
        BOX_LOAD(pb, pbox);
        BOX_STORE(mbox, mine);
        BOX_WAIT(1, pb);
        if (!box_full(pb)) {
          DRAIN();
          BOX_LOAD(pb, pbox);
          BOX_WAIT(0, pb);
          if (!box_full(pb)) return;
        }
      }
    } else {
      pb = u32x4{0x7FC00000u, 0x7FC00000u, 0x7FC00000u, 0x7FC00000u};
    }

    const f32x4 c = __builtin_bit_cast(f32x4, pb);
    const float M1 = dpp<ODD>(c[2]), L1 = dpp<ODD>(c[3]), L0 = dpp<ODD>(o[3]);
    const float Mf = fmaxf(M, M1), w0 = __builtin_amdgcn_exp2f(M - Mf), w1 = __builtin_amdgcn_exp2f(M1 - Mf);
    float t0 = w0 * L0, t1 = w1 * L1;
    asm volatile("" : "+v"(t0), "+v"(t1));
    const float inv = 1.0f / (t0 + t1);
    unsigned h[4];
#pragma unroll
    for (int i = 0; i < 4; ++i) {
      float u0 = w0 * o[i], u1 = w1 * c[i];
      asm volatile("" : "+v"(u0), "+v"(u1));
      h[i] = __builtin_bit_cast(unsigned short, __float2bfloat16((u0 + u1) * inv));
    }
    *reinterpret_cast<unsigned*>(op0) = h[0] | (h[1] << 16);
    if ((lane & 1) == 0) *reinterpret_cast<unsigned*>(op1) = h[2] | (h[3] << 16);
    return;
  }

  const int r = lane >> 3, c = lane & 7;
  const auto rk = __builtin_amdgcn_make_buffer_rsrc((void*)k, 0, KVBYTES, 0x00020000);
  const auto rv = __builtin_amdgcn_make_buffer_rsrc((void*)v, 0, KVBYTES, 0x00020000);
  const unsigned wbase = (unsigned)(wg * KEYS_PER_WG + wave * TILE) * ROWB;
  const unsigned loff = r * ROWB + c * 16;
  const unsigned j0 = (((((unsigned)bh * 2654435761u) >> 16) * (unsigned)T) >> 16) + (wg & 1) * (T / 2);
#define TILE_OFF(t) (wbase + ((j0 + (t)) & (T - 1)) * ROUNDB)

  u32x4 qf[3], ra[3], rb[3];
#pragma unroll
  for (int j = 0; j < 3; ++j) qf[j] = *reinterpret_cast<const u32x4*>(q + (size_t)bh * ROWB + j * 128 + c * 16);
  SB();
#define LOAD3(dst, rsrc, so) do { const unsigned so_ = (so); _Pragma("unroll") for (int j_ = 0; j_ < 3; ++j_) { \
    dst[j_] = __builtin_amdgcn_raw_buffer_load_b128(rsrc, loff + j_ * 128, so_, NT_LOAD); SB(); } } while (0)
  LOAD3(ra, rk, TILE_OFF(0));
  LOAD3(rb, rk, TILE_OFF(1));
  asm volatile("s_barrier" ::: "memory");

  float* sbuf = &lds[WAVES * REC + 4 + wave * (T * TILE) + r];
  float m = -INFINITY, sc;
#define QK(kr) do { float s0 = 0.f, s1 = 0.f; \
    _Pragma("unroll") for (int j_ = 0; j_ < 3; ++j_) { \
      s0 = dot2(kr[j_][0], qf[j_][0], s0); s1 = dot2(kr[j_][1], qf[j_][1], s1); \
      s0 = dot2(kr[j_][2], qf[j_][2], s0); s1 = dot2(kr[j_][3], qf[j_][3], s1); } \
    sc = s0 + s1; asm volatile("" : "+v"(sc)); SB(); } while (0)
#define SCORE(t) do { float s_ = sc; s_ += dpp<XOR1>(s_); s_ += dpp<XOR2>(s_); s_ += dpp<XOR7>(s_); s_ *= C2; \
    m = fmaxf(m, s_); sbuf[(t) * TILE] = s_; SB(); } while (0)
  for (int t = 0; t < T - 2; t += 2) {
    QK(ra); __builtin_amdgcn_s_sleep(K_PACE); LOAD3(ra, rk, TILE_OFF(t + 2)); SCORE(t);
    QK(rb); __builtin_amdgcn_s_sleep(K_PACE); LOAD3(rb, rk, TILE_OFF(t + 3)); SCORE(t + 1);
  }
  QK(ra); LOAD3(ra, rv, TILE_OFF(0)); SCORE(T - 2);
  QK(rb); LOAD3(rb, rv, TILE_OFF(1)); SCORE(T - 1);
  m = xrow_max(m);

  float l = 0.f, s_cur = sbuf[0];
  f32x2 acc[12];
#pragma unroll
  for (int i = 0; i < 12; ++i) acc[i] = f32x2{0.f, 0.f};
#define PV(vr, tnext) do { const float p_ = __builtin_amdgcn_exp2f(s_cur - m); s_cur = sbuf[(tnext) * TILE]; l += p_; const f32x2 p2_ = {p_, p_}; \
    _Pragma("unroll") for (int j_ = 0; j_ < 3; ++j_) _Pragma("unroll") for (int e_ = 0; e_ < 4; ++e_) { \
      const unsigned w_ = vr[j_][e_]; const f32x2 v2_ = {lo16(w_), hi16(w_)}; \
      acc[j_ * 4 + e_] = __builtin_elementwise_fma(p2_, v2_, acc[j_ * 4 + e_]); } \
    asm volatile("" : "+v"(acc[0]), "+v"(acc[1]), "+v"(acc[2]), "+v"(acc[3]), "+v"(acc[4]), "+v"(acc[5]), \
                      "+v"(acc[6]), "+v"(acc[7]), "+v"(acc[8]), "+v"(acc[9]), "+v"(acc[10]), "+v"(acc[11])); SB(); } while (0)
  for (int t = 0; t < T - V_DEEP; t += 2) {
    PV(ra, t + 1); LOAD3(ra, rv, TILE_OFF(t + 2));
    PV(rb, t + 2); LOAD3(rb, rv, TILE_OFF(t + 3));
  }
  u32x4 rc[3];
  LOAD3(rc, rv, TILE_OFF(T - V_DEEP + 2));
#pragma unroll 1
  for (int t = T - V_DEEP; t < T - 3; t += 3) {
    PV(ra, t + 1); LOAD3(ra, rv, TILE_OFF(t + 3));
    PV(rb, t + 2); LOAD3(rb, rv, TILE_OFF(t + 4));
    PV(rc, t + 3); LOAD3(rc, rv, TILE_OFF(t + 5));
  }
  PV(ra, T - 2);
  PV(rb, T - 1);
  PV(rc, T - 1);

  l = xrow_sum(l);
  float y[12], z[6];
#pragma unroll
  for (int i = 0; i < 12; ++i)
    y[i] = swap32_add(acc[i / 2][i % 2], acc[(i + 12) / 2][i % 2]);
#pragma unroll
  for (int i = 0; i < 6; ++i) z[i] = swap16_add(y[i], y[i + 6]);
#pragma unroll
  for (int i = 0; i < 6; ++i) z[i] += dpp<XOR8>(z[i]);
  const bool hi = lane & 8;
  const f32x4 rec = {hi ? z[4] : z[0], hi ? z[5] : z[1], hi ? m : z[2], hi ? l : z[3]};
  *reinterpret_cast<f32x4*>(&lds[wave * REC + (8 * (lane >> 4) + c) * 8 + (lane & 8) / 2]) = rec;
  asm volatile("" ::: "memory");
  if (lane == 0) __hip_atomic_fetch_add((__attribute__((address_space(3))) unsigned*)done, 1u, __ATOMIC_RELAXED, __HIP_MEMORY_SCOPE_WORKGROUP);
}

extern "C" void kernel_launch(void* const* d_in, const int* in_sizes, int n_in, void* d_out, int out_size, void* d_ws,
                              size_t ws_size, hipStream_t stream) {
  attn_decode<<<NWG, NTHR, 0, stream>>>((const unsigned char*)d_in[0], (const unsigned char*)d_in[1], (const unsigned char*)d_in[2],
                                        (unsigned char*)d_ws, (unsigned char*)d_out);
}
```
